```python
import torch

def _softmax(x, axis=-1):
    return torch.softmax(x, dim=axis)
def _gelu(x):
    return torch.nn.functional.gelu(x, approximate="tanh")
def _silu(x):
    return torch.nn.functional.silu(x)
def _sigmoid(x):
    return torch.sigmoid(x)
def _layernorm(x, axis=-1, eps=1e-5):
    m = x.mean(dim=axis, keepdim=True)
    v = x.var(dim=axis, keepdim=True, unbiased=False)
    return (x - m) / torch.sqrt(v + eps)
def _scatter_add(tgt, idx, src, axis=0):
    return tgt.clone().index_add_(axis, idx, src)
def _segment_sum(src, seg, n):
    out = torch.zeros((n,) + src.shape[1:], dtype=src.dtype)
    return out.index_add_(0, seg, src)
def _pad(x, pw):
    flat = tuple(v for pair in reversed(pw) for v in pair)
    return torch.nn.functional.pad(x, flat)

kernel_name = 'gated_mix__ssm_conv_s5__swiglu_ffn__prompt'

def setup_inputs():

    _w = (lambda a: (lambda t: t.to(torch.bfloat16) if t.is_floating_point() else t)(torch.as_tensor(a)))
    def _A_setup():
        B, S, D = 1, 2048, 512
        P = D
        Lambda_re = -0.5 * (lambda _x: torch.exp(torch.as_tensor(_x)))((lambda *s: torch.randn(*s, dtype=torch.bfloat16) * 0.2)(P))
        Lambda_im = (lambda *s: torch.randn(*s, dtype=torch.bfloat16) * 0.4)(P) * 1.0
        B_re = (lambda *s: torch.randn(*s, dtype=torch.bfloat16) * 0.2)(P, D) * (D ** -0.5)
        B_im = (lambda *s: torch.randn(*s, dtype=torch.bfloat16) * 0.2)(P, D) * (D ** -0.5)
        C_re = (lambda *s: torch.randn(*s, dtype=torch.bfloat16) * 0.2)(D, P) * (P ** -0.5)
        C_im = (lambda *s: torch.randn(*s, dtype=torch.bfloat16) * 0.2)(D, P) * (P ** -0.5)
        log_dt = (lambda *s: torch.randn(*s, dtype=torch.bfloat16) * 0.2)(P) * 0.3 - 2.0
        Dskip = (lambda *s: torch.randn(*s, dtype=torch.bfloat16) * 0.05)(D)
        return {
            "x": _w((lambda *s: torch.randn(*s, dtype=torch.bfloat16) * 0.05)(B, S, D)),
            "Lambda_re": _w(Lambda_re),
            "Lambda_im": _w(Lambda_im),
            "B_re": _w(B_re),
            "B_im": _w(B_im),
            "C_re": _w(C_re),
            "C_im": _w(C_im),
            "log_dt": _w(log_dt),
            "Dskip": _w(Dskip),
        }
    _A_in = _A_setup()

    def _B_setup():
        B, S, D, F = 1, 2048, 512, 1792
        return {
            "x": _w((lambda *s: torch.randn(*s, dtype=torch.bfloat16) * 0.05)(B, S, D)),
            "w_gate": _w((lambda *s: torch.randn(*s, dtype=torch.bfloat16) * 0.05)(D, F)),
            "w_up": _w((lambda *s: torch.randn(*s, dtype=torch.bfloat16) * 0.05)(D, F)),
            "w_down": _w((lambda *s: torch.randn(*s, dtype=torch.bfloat16) * 0.05)(F, D)),
        }
    _B_in = _B_setup()

    return {
        "A_x": _A_in["x"],
        "A_Lambda_re": _A_in["Lambda_re"],
        "A_Lambda_im": _A_in["Lambda_im"],
        "A_B_re": _A_in["B_re"],
        "A_B_im": _A_in["B_im"],
        "A_C_re": _A_in["C_re"],
        "A_C_im": _A_in["C_im"],
        "A_log_dt": _A_in["log_dt"],
        "A_Dskip": _A_in["Dskip"],
        "B_x": _B_in["x"],
        "B_w_gate": _B_in["w_gate"],
        "B_w_up": _B_in["w_up"],
        "B_w_down": _B_in["w_down"],
        "gate_w": _w((lambda *s: torch.randn(*s, dtype=torch.bfloat16) * 0.05)(512) * 16.0),
    }

def reference(A_x, A_Lambda_re, A_Lambda_im, A_B_re, A_B_im, A_C_re, A_C_im, A_log_dt, A_Dskip, B_x, B_w_gate, B_w_up, B_w_down, gate_w):
    def _A_ref(x, Lambda_re, Lambda_im, B_re, B_im, C_re, C_im, log_dt, Dskip):
        Bsz, L, H = x.shape
        Lam = Lambda_re + 1j * Lambda_im
        Bmat = B_re + 1j * B_im
        Cmat = C_re + 1j * C_im
        dt = torch.exp(log_dt)
        Lam_bar = torch.exp(dt * Lam)
        B_bar = ((Lam_bar - 1.0) / Lam)[:, None] * Bmat
        Bu = torch.einsum("bld,pd->blp", x + 0j, B_bar)
        h = torch.zeros_like(Bu[:, 0, :])
        hs = []
        for t in range(L):
            h = Lam_bar[None, :] * h + Bu[:, t, :]
            hs.append(h)
        hseq = torch.stack(hs, 1)
        y = 2.0 * torch.einsum("blp,dp->bld", hseq, Cmat).real + x * Dskip[None, None, :]
        return y

    def _B_ref(x, w_gate, w_up, w_down):
        g = _silu(x @ w_gate)
        u = x @ w_up
        return (g * u) @ w_down

    x = A_x
    g = _sigmoid((x * gate_w).sum(-1, keepdims=True))
    a = _A_ref(x, A_Lambda_re, A_Lambda_im, A_B_re, A_B_im, A_C_re, A_C_im, A_log_dt, A_Dskip)
    b = _B_ref(x, B_w_gate, B_w_up, B_w_down)
    return g * a + (1 - g) * b
```

```cpp
#include <hip/hip_runtime.h>
#include <hip/hip_bf16.h>
#include <stdint.h>
#include <type_traits>

#define LAS __attribute__((address_space(3)))
#define DI __device__ __forceinline__
#define AI __attribute__((always_inline))
typedef unsigned u32;
typedef u32 u32x4 __attribute__((ext_vector_type(4)));
typedef u32 u32x2 __attribute__((ext_vector_type(2)));
typedef short s16x4 __attribute__((ext_vector_type(4)));
typedef __bf16 bf16x8 __attribute__((ext_vector_type(8)));
typedef __bf16 bf16x2 __attribute__((ext_vector_type(2)));
typedef float f32x16 __attribute__((ext_vector_type(16)));
typedef float f32x4 __attribute__((ext_vector_type(4)));
typedef float f32x2 __attribute__((ext_vector_type(2)));
typedef __amdgpu_buffer_rsrc_t rsrc_t;

constexpr u32 OFF_FLAG = 4096, OFF_E = 65536, OFF_G = 196608, OFF_ACT = 262144, TBSTRIDE = 176 * 1024;
constexpr u32 WS_BYTES = OFF_ACT + 64 * TBSTRIDE;
constexpr u32 LDS_BYTES = 160 * 1024;
constexpr u32 kSpinCap = 1u << 16;

constexpr int WN[2][40] = {{12,12,12,16,0,0,0,0,0,0,0,0,0,20,9,36,27,6,22,10,16,10,15,13,12,0,0,0,0,0,13,12,25,0,0,0,0,0,0,0},
                           {0,0,0,0,0,0,0,0,0,24,0,12,0,0,0,0,4,5,6,11,12,4,5,5,4,0,0,0,0,0,0,0,4,4,16,0,0,0,0,0}};

template <int I, int N, class Fn> DI void sfor_(Fn&& f) { if constexpr (I < N) { f(std::integral_constant<int, I>{}); sfor_<I + 1, N>(f); } }
template <int N, class Fn> DI void sfor(Fn&& f) { sfor_<0, N>(f); }

#define SB() __builtin_amdgcn_sched_barrier(0)
#define MARK(id) asm volatile("; MARK %0" ::"i"(id) : "memory")
enum { M_SMALL = 200, M_GATH, M_HST, M_ACTA, M_ACTB, M_POLLH, M_POLLA, M_POLLB, M_MISC, M_A0 = 210  , M_NONE = 999 };
template <int V> using IC = std::integral_constant<int, V>;

template <int G, int ID, int N1, int N2 = M_NONE, int N3 = M_NONE, int N4 = M_NONE> DI void waitv() {
  asm volatile("s_waitcnt vmcnt(%0) ; WAIT %1 %2 NEED %3 %4 %5 %6" ::"i"(WN[G][ID]), "i"(G), "i"(ID), "i"(N1), "i"(N2), "i"(N3), "i"(N4) : "memory");
}
template <int N> DI void barl() { asm volatile("s_waitcnt lgkmcnt(%0)\n\ts_barrier" ::"i"(N) : "memory"); }
DI void bar() { asm volatile("s_waitcnt lgkmcnt(0)\n\ts_barrier" ::: "memory"); }

template <int ID, u32 LDSOFF, u32 SOFF, u32 D2> DI void dma4(u32 wbase, u32 v0, u32 v1, rsrc_t rs) {
  u32 sa, sb;
  asm volatile("s_add_u32 m0, %2, %3\n\ts_mov_b32 %0, %4\n\ts_mov_b32 %1, %5\n\t"
               "buffer_load_dwordx4 %6, %8, %0 offen lds\n\tbuffer_load_dwordx4 %7, %8, %0 offen offset:1024 lds\n\t"
               "buffer_load_dwordx4 %6, %8, %1 offen offset:2048 lds\n\tbuffer_load_dwordx4 %7, %8, %1 offen offset:3072 lds ; MARK %9"
               : "=&s"(sa), "=&s"(sb) : "s"(wbase), "i"(LDSOFF), "i"(SOFF), "i"(SOFF + D2), "v"(v0), "v"(v1), "s"(rs), "i"(ID) : "scc", "memory");
}
template <int ID, u32 LDSOFF, u32 SOFF> DI void dma2(u32 wbase, u32 v0, u32 v1, rsrc_t rs) {
  u32 sa;
  asm volatile("s_add_u32 m0, %1, %2\n\ts_mov_b32 %0, %3\n\t"
               "buffer_load_dwordx4 %4, %6, %0 offen lds\n\tbuffer_load_dwordx4 %5, %6, %0 offen offset:1024 lds ; MARK %7"
               : "=&s"(sa) : "s"(wbase), "i"(LDSOFF), "i"(SOFF), "v"(v0), "v"(v1), "s"(rs), "i"(ID) : "scc", "memory");
}
template <int OFF> DI u32x4 ld128_sc1(u32 voff, rsrc_t rs, u32 soff) {
  u32x4 r; asm volatile("buffer_load_dwordx4 %0, %1, %2, %3 offen offset:%4 sc1" : "=v"(r) : "v"(voff), "s"(rs), "s"(soff), "i"(OFF) : "memory"); return r; }
template <int OFF> DI u32x4 ld128(u32 voff, rsrc_t rs, u32 soff) {
  u32x4 r; asm volatile("buffer_load_dwordx4 %0, %1, %2, %3 offen offset:%4" : "=v"(r) : "v"(voff), "s"(rs), "s"(soff), "i"(OFF) : "memory"); return r; }
template <int OFF> DI u32x2 ld64_sc1(u32 voff, rsrc_t rs) {
  u32x2 r; asm volatile("buffer_load_dwordx2 %0, %1, %2, 0 offen offset:%3 sc1" : "=v"(r) : "v"(voff), "s"(rs), "i"(OFF) : "memory"); return r; }
template <int OFF> DI u32x2 ld64(u32 voff, rsrc_t rs) {
  u32x2 r; asm volatile("buffer_load_dwordx2 %0, %1, %2, 0 offen offset:%3" : "=v"(r) : "v"(voff), "s"(rs), "i"(OFF) : "memory"); return r; }
DI u32 ld32_sc1(u32 voff, rsrc_t rs) { u32 r; asm volatile("buffer_load_dword %0, %1, %2, 0 offen sc1" : "=v"(r) : "v"(voff), "s"(rs) : "memory"); return r; }
DI u32 ld16(u32 voff, rsrc_t rs) { u32 r; asm volatile("buffer_load_ushort %0, %1, %2, 0 offen" : "=v"(r) : "v"(voff), "s"(rs) : "memory"); return r; }
DI u32x2 ld64_slow(u32 voff, rsrc_t rs) {
  u32x2 r; asm volatile("buffer_load_dwordx2 %0, %1, %2, 0 offen sc1 ; SLOW\n\ts_waitcnt vmcnt(0) ; SLOW" : "=&v"(r) : "v"(voff), "s"(rs) : "memory"); return r; }
DI u32x4 ld128_slow(u32 voff, rsrc_t rs) {
  u32x4 r; asm volatile("buffer_load_dwordx4 %0, %1, %2, 0 offen sc1 ; SLOW\n\ts_waitcnt vmcnt(0) ; SLOW" : "=&v"(r) : "v"(voff), "s"(rs) : "memory"); return r; }
DI u32x4 ld128_slow_nw(u32 voff, rsrc_t rs) {
  u32x4 r; asm volatile("buffer_load_dwordx4 %0, %1, %2, 0 offen sc1 ; SLOW" : "=v"(r) : "v"(voff), "s"(rs) : "memory"); return r; }
DI void st128_sc1(u32x4 v, u32 voff, rsrc_t rs, u32 soff) { asm volatile("buffer_store_dwordx4 %0, %1, %2, %3 offen sc1\n\ts_nop 1" ::"v"(v), "v"(voff), "s"(rs), "s"(soff) : "memory"); }
template <int OFF> DI void st128(u32x4 v, u32 voff, rsrc_t rs) { asm volatile("buffer_store_dwordx4 %0, %1, %2, 0 offen offset:%3 sc1\n\ts_nop 1" ::"v"(v), "v"(voff), "s"(rs), "i"(OFF) : "memory"); }
DI void st64_sc1(u32x2 v, u32 voff, rsrc_t rs) { asm volatile("buffer_store_dwordx2 %0, %1, %2, 0 offen sc1" ::"v"(v), "v"(voff), "s"(rs) : "memory"); }
DI void st32_cond(u32 v, u32 voff, rsrc_t rs) { asm volatile("buffer_store_dword %0, %1, %2, 0 offen sc1 ; COND\n\ts_nop 1" ::"v"(v), "v"(voff), "s"(rs) : "memory"); }
template <class T> DI void pin(T& a) { asm volatile("" : "+v"(a)); }
DI u32 opq(u32 v) { asm("" : "+v"(v)); return v; }
DI float dot2(u32 a, u32 b, float c) { return __builtin_amdgcn_fdot2_f32_bf16(__builtin_bit_cast(bf16x2, a), __builtin_bit_cast(bf16x2, b), c, false); }

DI u32x4 lds128(u32 a) { return *(LAS u32x4*)(uintptr_t)a; }
DI u32x4 ldstr(u32 a0, u32 a1) {
  s16x4 lo = __builtin_amdgcn_ds_read_tr16_b64_v4i16((LAS s16x4*)(uintptr_t)a0);
  s16x4 hi = __builtin_amdgcn_ds_read_tr16_b64_v4i16((LAS s16x4*)(uintptr_t)a1);
  u32x2 l = __builtin_bit_cast(u32x2, lo), h = __builtin_bit_cast(u32x2, hi);
  return (u32x4){l.x, l.y, h.x, h.y};
}
DI f32x16 mfma(u32x4 a, u32x4 b, f32x16 c) {
  return __builtin_amdgcn_mfma_f32_32x32x16_bf16(__builtin_bit_cast(bf16x8, a), __builtin_bit_cast(bf16x8, b), c, 0, 0, 0);
}
DI u32 pk(float a, float b) { f32x2 v = {a, b}; return __builtin_bit_cast(u32, __builtin_convertvector(v, bf16x2)); }
DI float bf2f(u32 v) { return __uint_as_float(v << 16); }
DI float rbf(float a) { return bf2f(pk(a, a) & 0xffffu); }
DI float silu_mul(float g, float u) { return g * u * __builtin_amdgcn_rcpf(1.f + __builtin_amdgcn_exp2f(g * -1.44269504089f)); }
DI rsrc_t mk(const void* p, u32 bytes) { return __builtin_amdgcn_make_buffer_rsrc((void*)p, 0, bytes, 0x00020000); }
struct cpx { float r, i; };
DI cpx cmul(cpx a, cpx b) { return {a.r * b.r - a.i * b.i, a.r * b.i + a.i * b.r}; }
DI cpx cfma(cpx a, cpx b, cpx c) { return {__builtin_fmaf(-a.i, b.i, __builtin_fmaf(a.r, b.r, c.r)), __builtin_fmaf(a.i, b.r, __builtin_fmaf(a.r, b.i, c.i))}; }

struct cpn { float r, i, n; };
DI cpn mkn(cpx a) { cpn v = {a.r, a.i, -a.i}; asm volatile("" : "+v"(v.r), "+v"(v.i), "+v"(v.n)); return v; }
DI cpx cfma(cpn a, cpx b, cpx c) { return {__builtin_fmaf(a.n, b.i, __builtin_fmaf(a.r, b.r, c.r)), __builtin_fmaf(a.i, b.r, __builtin_fmaf(a.r, b.i, c.i))}; }
DI cpx cmul(cpn a, cpx b) { return {__builtin_fmaf(a.n, b.i, a.r * b.r), __builtin_fmaf(a.i, b.r, a.r * b.i)}; }
DI u32 touch(u32 voff, rsrc_t rs) { return ld32_sc1(voff, rs); }
DI void keep(u32 v) { asm volatile("" ::"v"(v)); }
constexpr u32 OOB = 0x40000000u;
constexpr u32 CODE_AHEAD = 30208;

constexpr int step_slot(int t) { return (t - 8 + 2) % 5; }
constexpr u32 step_base(int t) { return t < 8 ? (t % 6) * 24576 : step_slot(t) * 32768; }
constexpr int last_unit(int step) { return 2 * step + 1; }
constexpr u32 LDS_AGG = 7 * 8192, LDS_SCR = 18 * 8192;

constexpr int RQ_END = 34;
constexpr int PA = 1, FB = 3, PB = 5;
constexpr int NRL = 16, RLIST[NRL] = {2, 104, 3, 105, 4, 5, 6, 106, 7, 107, 108, 8, 9, 109, 10, 110};
constexpr int rq_e(int q) { return q / 8 == 2 ? RLIST[q / 4] : RLIST[2 * (q / 8) + q % 2]; }
constexpr int rq_i(int q) { return q / 8 == 2 ? q % 4 : (q % 8) / 2; }
constexpr int rq(int k) {
  if (k < 0) return 0;
  int v = (4 * NRL * (k + 1) + RQ_END) / (RQ_END + 1);
  if (v > 4 * NRL) v = 4 * NRL;
  const int freed = (k + 1) / 4 - 1;
  for (int q = 0; q < 4 * NRL; ++q) {
    const int e = rq_e(q);
    const bool ok = e >= 100 ? e - 105 <= freed : e >= 8 ? freed >= PB : e >= 4 ? freed >= PA : true;
    if (!ok && v > q) v = q;
  }
  return v;
}

template <int G>
DI void body(const void* px, const void* pLre, const void* pLim, const void* pBre, const void* pBim, const void* pCre, const void* pCim,
             const void* pldt, const void* pDsk, const void* pwg, const void* pwu, const void* pwd, const void* pgw, float* pout, void* pws, const u32 w, const u32 tc) {
  asm volatile("; BODY_BEGIN %0" ::"i"(G));
  const u32 lane = threadIdx.x & 63, n = lane & 31, h = lane >> 5;
  const u32 b = blockIdx.x, xcd = b & 7, idx = b >> 3;
  const u32 r = 8 * (xcd & 1) + (idx & 7), c = 4 * (xcd >> 1) + (idx >> 3);
  const u32 tb = w & 3, wbase = tb * 4096, t1 = tb & 1, t2 = tb >> 1;
  const u32 sg = c >> 2, i4 = c & 3, cb = c & 7;
  const bool heavy = i4 < 3;
  const void* const pB = t1 ? pBim : pBre;
  const void* const pF = t2 ? pwu : pwg;
  const rsrc_t rsX = mk(px, 2048 * 1024), rsB = mk(pB, 512 * 1024);
  const rsrc_t rsW = mk(pws, WS_BYTES);
  const rsrc_t rsF = mk(pF, 512 * 3584), rsF2 = mk(pF, heavy ? 512 * 3584 : 0);
  const rsrc_t rsD = mk(pwd, 1792 * 1024), rsCr = mk(pCre, 512 * 1024), rsCi = mk(pCim, 512 * 1024);
  const u32 l3 = lane >> 3, swz = 16 * ((lane & 7) ^ (lane >> 4));
  const u32 voffX = (128 * r + 32 * tb + 16 * h + (l3 & 3)) * 1024 + swz, voffX1 = (voffX ^ 64) + 3072;
  const u32 voffB = (32 * c + l3) * 1024 + swz + 128 * t2, voffB1 = (voffB ^ 64) + 7168, wbaseB = t2 * 24576 + t1 * 4096;
  const u32 voffF = (32 * t1 + l3) * 3584 + 2 * (448 * sg + 128 * i4) + 16 * ((lane & 7) ^ (2 * ((lane >> 4) & 1) + 4 * t2)), voffF1 = voffF + 27648;
  const u32 voffC = (64 * cb + 32 * t1 + l3) * 1024 + swz + 128 * t2, voffC1 = (voffC ^ 64) + 7168;
  const u32 voffD = (32 * t1 + l3 + 128 * t2) * 1024 + 128 * cb + 16 * ((lane & 7) ^ (4 * ((lane >> 4) & 1))), voffE2 = voffD + t2 * 320 * 1024;

  auto issueP = [&](auto ID, auto Pc) AI {
    constexpr int id = ID, t = id / 2, hf = id % 2, p = Pc; constexpr u32 L = step_base(t) + hf * 16384;
    auto go = [&](auto SO, auto DL, u32 wb, u32 v0, u32 v1, rsrc_t rs) AI {
      if constexpr (p == 2) dma4<id, L, SO, 2 * DL>(wb, v0, v1, rs);
      else dma2<id, L + p * 2048, SO + p * (2 * DL + 2048)>(wb, v0, v1, rs);
    };
    if constexpr (t < 8) {
      if constexpr (hf == 0) go(IC<t * 128>{}, IC<3072>{}, wbase, voffX, voffX1, rsX);
      else go(IC<t * 128>{}, IC<7168>{}, wbaseB, voffB, voffB1, rsB);
    } else if constexpr (t < 12) go(IC<(128 * (t - 8) + 64 * hf) * 3584>{}, IC<27648>{}, wbase, voffF, voffF1, rsF);
    else if constexpr (t < 16) go(IC<(128 * (t - 12) + 64 * hf) * 3584 + 128>{}, IC<27648>{}, wbase, voffF, voffF1, rsF2);
    else if constexpr (t < 18) go(IC<(4 * (t - 16) + 2 * hf) * 128>{}, IC<7168>{}, wbase, voffC, voffC1, rsCr);
    else if constexpr (t < 20) go(IC<(4 * (t - 18) + 2 * hf) * 128>{}, IC<7168>{}, wbase, voffC, voffC1, rsCi);
    else if constexpr (t < 24) go(IC<(448 * (t - 20) + 256 * hf) * 1024>{}, IC<7168>{}, wbase, voffD, voffD + 7168, rsD);
    else go(IC<(896 * hf + 64 + 128 * (t - 24)) * 1024>{}, IC<7168>{}, wbase, voffE2, voffE2 + 7168, rsD);
  };
  auto issue = [&](auto ID) AI { issueP(ID, IC<2>{}); };

  const u32 rdA = n * 128 + 16 * (h ^ ((n >> 1) & 7));
  const u32 q4 = (lane >> 2) & 3, p4 = lane & 3, a1 = (lane >> 4) & 1;
  const u32 rdF = a1 * 8192 + (8 * h + q4) * 128 + 16 * ((p4 >> 1) ^ (2 * (q4 >> 1) + 4 * a1)) + 8 * (p4 & 1);
  const u32 voffAct = OFF_ACT + (4 * r + tb) * TBSTRIDE + lane * 16;
  const u32 tb2 = w & 1, ks = w >> 1, TB = 4 * r + 2 * (c >> 3) + tb2;
  const u32 flagP = OFF_FLAG + (4 * r + tb) * 768 + c * 8;
  const u32 flagC = OFF_FLAG + TB * 768 + (lane & 15) * 8;
  const u32 voffA = OFF_ACT + TB * TBSTRIDE + ks * 4096 + lane * 16;
  u32 tag = 0; u32x2 tag2;
  f32x16 acc[4] = {};
  u32x4 af[44];
  u32x2 pollH, pollA, pollB;
  auto loadA1 = [&](auto K) AI { constexpr int k = K; af[k] = ld128_sc1<(k & 3) * 1024>(voffA, rsW, (k >> 2) * 16384); if constexpr ((k & 3) == 3) MARK(M_A0 + (k >> 2)); };
  auto loadA = [&](auto R) AI { sfor<4>([&](auto I) AI { loadA1(IC<4 * R + I>{}); }); };
  auto flag = [&](u32 off) AI { st64_sc1(tag2, lane == 0 ? off : +OOB, rsW); };
  auto pinA = [&](auto R) AI { constexpr int rho = R; pin(af[4 * rho]); pin(af[4 * rho + 1]); pin(af[4 * rho + 2]); pin(af[4 * rho + 3]); };
  auto check = [&](u32x2 v, u32 voff) AI {
    pin(v);
    if (__builtin_expect(__builtin_amdgcn_ballot_w64(v.x != tag2.x || v.y != tag2.y) != 0, 0)) {
#pragma unroll 1
      for (u32 it = 0; it < kSpinCap; ++it) {
        v = ld64_slow(voff, rsW);
        if (__builtin_amdgcn_ballot_w64(v.x != tag2.x || v.y != tag2.y) == 0) break;
        __builtin_amdgcn_s_sleep(1);
      }
    }
    asm volatile("" ::: "memory");
  };
  auto silu_store = [&](auto U, u32 voff, u32 soff, auto&& hook) AI {
    u32x4 fr;
    sfor<4>([&](auto I) AI {
      fr[(int)I] = pk(silu_mul(acc[U][2 * I], acc[U][2 * I + 8]), silu_mul(acc[U][2 * I + 1], acc[U][2 * I + 9]));
      if constexpr (I == 1) { pin(fr); SB(); hook(IC<0>{}); SB(); }
    });
    st128_sc1(fr, voff, rsW, soff);
    SB(); hook(IC<1>{}); SB();
  };

  u32x4 xf[32], wf[2][4];
  auto rdF1 = [&](auto K, auto V) AI {
    constexpr int k = K, v = V; constexpr u32 o = ((k >> 2) & 1) * 16384 + (k & 3) * 2048;
    const u32 a = opq((rdF ^ (32 * v)) + step_slot(8 + 4 * G + (k >> 3)) * 32768);
    wf[k & 1][v] = ldstr(a + o, a + o + 512);
  };
  auto ffn_block = [&](auto&& boundary, auto&& extra) AI {
    __builtin_amdgcn_s_setprio(2);
    sfor<4>([&](auto V) AI { rdF1(IC<0>{}, V); });
    sfor<32>([&](auto K) AI {
      constexpr int k = K;
      sfor<4>([&](auto V) AI {
        if constexpr (G == 0 && k == 7) {
          if constexpr (V == 2) { boundary(IC<0>{}); bar(); SB(); sfor<4>([&](auto V2) AI { rdF1(IC<k + 1>{}, V2); }); }
        } else if constexpr (k < 31) rdF1(IC<k + 1>{}, V);
        SB();
        acc[V] = mfma(wf[k & 1][V], xf[k], acc[V]);
        pin(acc[V]);
        SB();
      });
      if constexpr (k % 8 == 7 && !(G == 0 && k == 7)) { boundary(IC<k / 8>{}); barl<8>(); SB(); }
      if constexpr (k % 2 == 1) { extra(K); SB(); }
    });
    __builtin_amdgcn_s_setprio(0);
  };
  auto tau = [](u32 q) { return 16 * ((q >> 2) & 1) + 4 * ((q >> 3) & 3) + (q & 3); };
  const u32 tok0 = 128 * r + 64 * (c >> 3);
  const u32 frow = 8 * w + l3, ftok = tok0 + (frow & 32) + tau(frow), fcol = 64 * cb + 4 * (lane & 7);
  u32x2 gq, xs0, xs1, ds0, ds1;
  auto load_misc = [&]() AI {
    gq = ld64_sc1<0>(OFF_G + (tok0 + 32 * tb2 + tau(n)) * 8, rsW);
    xs0 = ld64<0>(ftok * 1024 + fcol * 2, rsX); xs1 = ld64<64>(ftok * 1024 + fcol * 2, rsX);
    const rsrc_t rsK = mk(pDsk, 1024);
    ds0 = ld64<0>(fcol * 2, rsK); ds1 = ld64<64>(fcol * 2, rsK);
    MARK(M_MISC);
  };

  if constexpr (G == 0) {
    u32 tagv = ld32_sc1(0, rsW);
    MARK(M_SMALL);
    issue(IC<4>{}); issue(IC<5>{}); issue(IC<6>{});
    SB();
    waitv<G, 30, last_unit(1)>(); bar();
    SB();
    sfor<4>([&](auto T) AI {
      constexpr int t = T;
      sfor<8>([&](auto Sx) AI { constexpr int s = Sx; xf[8 * t + s] = lds128((rdA ^ (32 * (s & 3))) + tb * 4096 + step_base(2 * t + (s >> 2))); });
      SB();
      if constexpr (t == 0) { issue(IC<8>{}); issue(IC<9>{}); issue(IC<10>{}); }
      if constexpr (t == 1) { issue(IC<12>{}); issue(IC<13>{}); issue(IC<14>{}); }
      if constexpr (t == 2) { issue(IC<16>{}); issue(IC<17>{}); issue(IC<21>{}); }
      if constexpr (t == 3) { issue(IC<18>{}); issue(IC<19>{}); issue(IC<20>{}); }
      SB();
      waitv<G, t, last_unit(t < 3 ? 2 * t + 3 : 8), M_SMALL>(); bar();
      SB();
    });
    pin(tagv); keep(tc);
    tag = __builtin_amdgcn_readfirstlane(tagv) + 1; tag2 = (u32x2){tag, ~tag};
    SB();
    ffn_block([&](auto J) AI { SB(); if constexpr (J == 0) waitv<G, 8, last_unit(10)>(); }, [&](auto) AI {});
    SB();
    issue(IC<30>{});
    sfor<4>([&](auto U) AI {
      silu_store(U, voffAct, (64 + 4 * c + U) * 1024, [&](auto I) AI {
        constexpr int q = 2 * U + I;
        if constexpr (q == 7) MARK(M_ACTA);
        if constexpr (q == 0) issue(IC<31>{});
        if constexpr (q == 1) load_misc();
        if constexpr (q == 2) { SB(); bar(); SB(); }
        if constexpr (q >= 2 && q <= 4) issue(IC<30 + q>{});
        if constexpr (q == 5) { SB(); waitv<G, 13, last_unit(15)>(); bar(); SB(); pollH = ld64_sc1<512>(flagC, rsW); MARK(M_POLLH); issue(IC<35>{}); }
        if constexpr (q == 6) issue(IC<36>{});
      });
    });
    SB();
    waitv<G, 14, M_POLLH>();
    SB();
    check(pollH, flagC + 512);
    SB();
    loadA(IC<0>{});
    bar();
    SB();
    if (r == 15 && c == 0 && w == 0 && lane == 0) st32_cond(tag, 0, rsW);
    loadA(IC<1>{}); issue(IC<37>{});
    waitv<G, 31, M_ACTA>();
    flag(flagP);
    loadA(IC<2>{});
    issue(IC<38>{});
    SB();
    waitv<G, 15, last_unit(16)>(); bar();
    SB();
    issue(IC<39>{});
    loadA(IC<3>{});
  } else {
    __builtin_amdgcn_s_setprio(2);
    const u32 o = (32 * c + n) * 2;
    u32 sm0 = ld16(o, mk(pLre, 1024)), sm1 = ld16(o, mk(pLim, 1024)), sm2 = ld16(o, mk(pldt, 1024));
    u32 tagv = ld32_sc1(0, rsW);
    const u32 ox = (128 * r + 8 * c + 2 * tb) * 1024 + lane * 16;
    u32x4 xr0 = ld128<0>(ox, rsX, 0), xr1 = ld128<1024>(ox, rsX, 0), gwv = ld128<0>(lane * 16, mk(pgw, 1024), 0);
    MARK(M_SMALL);
    const void* const pp1 = tb < 2 ? pwg : pwu;
    const void* const pp2 = tb == 0 ? pCre : tb == 1 ? pCim : pwd;
    const u32 z2 = tb < 2 ? 512 * 1024 : 1792 * 1024, n2 = tb < 2 ? 16 : tb == 2 ? 32 : 24, b2 = tb < 2 ? 2 * b : 7 * b + 4 * (tb & 1);
    u32 tj[3];
    auto pf = [&](auto J) AI {
      if constexpr (J < 2) {
        const u32 l7 = lane / 7, v = (32 * r + 8 * i4 + 4 * (tb & 1) + l7) * 3584 + 896 * sg + 128 * (lane - 7 * l7);
        tj[J] = touch(lane < 28 && ((lane - 7 * l7) & 1) == J ? v : +OOB, mk(pp1, 512 * 3584));
      } else {
        const u32 v = b2 * 1024 + lane * 128;
        tj[2] = touch(lane < n2 ? v : +OOB, mk(pp2, z2));
      }
    };
    waitv<G, 30, M_SMALL>();
    pin(tagv); pin(sm0); pin(sm1); pin(sm2); pin(xr0); pin(xr1); pin(gwv); keep(tc);
    SB();
    tag = __builtin_amdgcn_readfirstlane(tagv) + 1; tag2 = (u32x2){tag, ~tag};
    cpx lam, coef;
    {
      const float lr = bf2f(sm0), li = bf2f(sm1), dt = rbf(__builtin_amdgcn_exp2f(bf2f(sm2) * 1.44269504089f));
      const float rv = dt * li * 0.15915494309f, rev = rv - __builtin_rintf(rv);
      const float sn = __builtin_amdgcn_sinf(rev), cs = __builtin_amdgcn_cosf(rev);
      const float e = __builtin_amdgcn_exp2f(dt * lr * 1.44269504089f);
      lam = {e * cs, e * sn};
      const float nr = lam.r - 1.f, ni = lam.i, den = 2.f * __builtin_amdgcn_rcpf(lr * lr + li * li);
      coef = {(nr * lr + ni * li) * den, (ni * lr - nr * li) * den};
    }
    cpx l16, l32, l128, lw;
    cpn nlam, ncoef, nl256;
    {
      cpx l2 = cmul(lam, lam), l4 = cmul(l2, l2), l8 = cmul(l4, l4);
      l16 = cmul(l8, l8); l32 = cmul(l16, l16);
      cpx l64 = cmul(l32, l32), l96 = cmul(l64, l32);
      l128 = cmul(l64, l64);
      lw = tb == 0 ? cpx{1.f, 0.f} : tb == 1 ? l32 : tb == 2 ? l64 : l96;
      cpx lw2 = cmul(lw, l16);
      if (h) lw = lw2;
      pin(l16.r); pin(l16.i); pin(l32.r); pin(l32.i); pin(l128.r); pin(l128.i); pin(lw.r); pin(lw.i);
      nlam = mkn(lam); ncoef = mkn(coef); nl256 = mkn(cmul(l128, l128));
    }
    auto gate = [&]() AI {
      float s0 = 0.f, s1 = 0.f;
      sfor<4>([&](auto J) AI {
        const u32 gw2 = gwv[(int)J], a = xr0[(int)J], b2 = xr1[(int)J];
        const float gl = bf2f(gw2), gh = __uint_as_float(gw2 & 0xffff0000u);
        s0 = dot2(pk(bf2f(a) * gl, __uint_as_float(a & 0xffff0000u) * gh), 0x3f803f80u, s0);
        s1 = dot2(pk(bf2f(b2) * gl, __uint_as_float(b2 & 0xffff0000u) * gh), 0x3f803f80u, s1);
      });
      auto sw = __builtin_amdgcn_permlane32_swap(__float_as_uint(s0), __float_as_uint(s1), false, false);
      float sv = __uint_as_float(sw[0]) + __uint_as_float(sw[1]);
      sv += __uint_as_float(__builtin_amdgcn_update_dpp(0, __float_as_uint(sv), 0xB1, 0xf, 0xf, true));
      sv += __uint_as_float(__builtin_amdgcn_update_dpp(0, __float_as_uint(sv), 0x4E, 0xf, 0xf, true));
      sv += __uint_as_float(__builtin_amdgcn_update_dpp(0, __float_as_uint(sv), 0x141, 0xf, 0xf, true));
      sv += __uint_as_float(__builtin_amdgcn_update_dpp(0, __float_as_uint(sv), 0x140, 0xf, 0xf, true));
      auto sx = __builtin_amdgcn_permlane16_swap(__float_as_uint(sv), __float_as_uint(sv), false, false);
      sv = rbf(__uint_as_float(sx[0]) + __uint_as_float(sx[1]));
      const float gg = rbf(__builtin_amdgcn_rcpf(1.f + __builtin_amdgcn_exp2f(sv * -1.44269504089f)));
      st64_sc1((u32x2){__float_as_uint(gg), tag}, n == 0 ? OFF_G + (128 * r + 8 * c + 2 * tb + h) * 8 : +OOB, rsW);
    };
    u32 vAgg = LDS_AGG + n * 8, vPub = (tb == 3 && r < 15 && h == 0) ? OFF_E + (14 - r) * 8192 + (32 * c + n) * 16 : +OOB;
    pin(vAgg); pin(vPub);
    u32x4 bk[4][2];
    f32x16 accR = {}, accI = {};
    auto rdS = [&](auto K) AI {
      constexpr int k = K;
      const u32 a = opq((rdA ^ (32 * (k & 3))) + step_base(k >> 2));
      xf[k] = lds128(a + tb * 4096); bk[k & 3][0] = lds128(a + 16384); bk[k & 3][1] = lds128(a + 20480);
    };
    pf(IC<0>{});
    SB();
    bar();
    SB();
    rdS(IC<0>{}); rdS(IC<1>{}); rdS(IC<2>{});
    sfor<32>([&](auto K) AI {
      constexpr int k = K;
      if constexpr (k == 16) pf(IC<1>{});
      if constexpr (k == 5) { gate(); SB(); }
      if constexpr (k % 8 == 5 && k < 28) { SB(); bar(); SB(); }
      if constexpr (k < 29) rdS(IC<k + 3>{});
      SB();
      accR = mfma(xf[k], bk[k & 3][0], accR); accI = mfma(xf[k], bk[k & 3][1], accI);
      SB();
    });
    __builtin_amdgcn_s_setprio(3);
    SB();
    float vr[16], vi[16];
    cpx e = {accR[0], accI[0]};
    sfor<15>([&](auto I) AI { e = cfma(nlam, e, cpx{accR[I + 1], accI[I + 1]}); });
    cpx elo, ehi;
    { auto sr = __builtin_amdgcn_permlane32_swap(__float_as_uint(e.r), __float_as_uint(e.r), false, false);
      auto si = __builtin_amdgcn_permlane32_swap(__float_as_uint(e.i), __float_as_uint(e.i), false, false);
      elo = {__uint_as_float(sr[0]), __uint_as_float(si[0])}; ehi = {__uint_as_float(sr[1]), __uint_as_float(si[1])}; }
    const cpx agg = cfma(l16, elo, ehi);
    *(LAS f32x2*)(uintptr_t)(vAgg + tb * 256) = (f32x2){agg.r, agg.i};
    bar();
    SB();
    const f32x2 g0 = *(LAS f32x2*)(uintptr_t)vAgg, g1_ = *(LAS f32x2*)(uintptr_t)(vAgg + 256), g2 = *(LAS f32x2*)(uintptr_t)(vAgg + 512);
    const cpx p1 = {g0.x, g0.y}, p2 = cfma(l32, p1, cpx{g1_.x, g1_.y}), p3 = cfma(l32, p2, cpx{g2.x, g2.y});
    {
      const cpx E = cfma(l32, p3, agg);
      st128_sc1((u32x4){__float_as_uint(E.r), tag, __float_as_uint(E.i), tag}, vPub, rsW, 0);
    }
    SB();
    pf(IC<2>{});
    issue(IC<22>{});
    cpx pw = tb == 0 ? cpx{0.f, 0.f} : tb == 1 ? p1 : tb == 2 ? p2 : p3;
    { cpx t = cfma(l16, pw, elo); if (h) pw = t; }
    pw = cmul(ncoef, pw); lw = cmul(ncoef, lw);
    float ncr = -ncoef.r; pin(ncr); pin(pw.r); pin(pw.i); pin(lw.r); pin(lw.i);
    SB(); issue(IC<23>{}); SB();
    const rsrc_t rsE = mk((const char*)pws + OFF_E, 15 * 8192);
    const u32 voffE = (15 - r + h) * 8192 + (32 * c + n) * 16;
    u32x4 ge[8];
    sfor<16>([&](auto I) AI {
      if constexpr (I == 8) { SB(); issue(IC<24>{}); SB(); }
      vr[I] = __builtin_fmaf(ncoef.n, accI[(int)I], ncoef.r * accR[(int)I]); vi[I] = __builtin_fmaf(ncoef.n, accR[(int)I], ncr * accI[(int)I]);
      pin(vr[I]); pin(vi[I]);
    });
    SB(); bar(); SB();
    sfor<8>([&](auto J) AI { ge[J] = ld128_sc1<0>(voffE + J * 16384, rsE, 0); });
    MARK(M_GATH);
    SB();
    issue(IC<25>{});
    u32 ex[8];
    sfor<8>([&](auto J) AI { ex[J] = 2 * J + h < r ? tag : 0u; pin(ex[J]); });
    SB(); issue(IC<26>{}); SB();
    u32 trw = opq(LDS_SCR + tb * 4096 + n * 64 + 16 * ((2 * h) ^ ((n >> 1) & 3))), trw2 = opq(trw ^ 16);
    u32 trr = opq(LDS_SCR + tb * 4096 + (8 * h + q4) * 64 + 16 * ((2 * (p4 & 1) + a1) ^ (q4 >> 1)) + 8 * (p4 >> 1)), trr2 = opq(trr ^ 32);
    u32 vAct = voffAct, rdFp = rdF;
    pin(trw); pin(trw2); pin(trr); pin(trr2); pin(vAct);
    asm volatile("" ::"v"(rdFp));
    SB(); issue(IC<27>{});
    SB();
    waitv<G, 9, last_unit(11)>(); keep(tj[0]); keep(tj[1]); keep(tj[2]); bar();
    SB();
    issue(IC<28>{});
    waitv<G, 34, M_GATH>();
    SB();
    sfor<8>([&](auto J) AI { pin(ge[J]); });
    auto gbad = [&]() AI {
      u32 bad = 0;
      sfor<8>([&](auto J) AI { bad |= opq((ge[J].y ^ ex[J]) | (ge[J].w ^ ex[J])); });
      return __builtin_amdgcn_ballot_w64(opq(bad) != 0) != 0;
    };
    if (__builtin_expect(gbad(), 0)) {
      SB();
#pragma unroll 1
      for (u32 it = 0; it < kSpinCap; ++it) {
        sfor<7>([&](auto J) AI { ge[J] = ld128_slow_nw(voffE + J * 16384, rsE); });
        ge[7] = ld128_slow(voffE + 7 * 16384, rsE);
        sfor<7>([&](auto J) AI { pin(ge[J]); });
        if (!gbad()) break;
      }
    }
    SB();
    cpx sacc = {__uint_as_float(ge[7].x), __uint_as_float(ge[7].z)};
    sfor<7>([&](auto J) AI { sacc = cfma(nl256, sacc, cpx{__uint_as_float(ge[6 - J].x), __uint_as_float(ge[6 - J].z)}); });
    cpx s0, s1;
    { auto sr = __builtin_amdgcn_permlane32_swap(__float_as_uint(sacc.r), __float_as_uint(sacc.r), false, false);
      auto si = __builtin_amdgcn_permlane32_swap(__float_as_uint(sacc.i), __float_as_uint(sacc.i), false, false);
      s0 = {__uint_as_float(sr[0]), __uint_as_float(si[0])}; s1 = {__uint_as_float(sr[1]), __uint_as_float(si[1])}; }
    cpx st = cfma(lw, cfma(l128, s1, s0), pw);
    st.i = -st.i;
    const cpn clam = {nlam.r, nlam.n, nlam.i};
    u32 hr[8], hi[8];
    sfor<8>([&](auto I) AI {
      cpx sa = cfma(clam, st, cpx{vr[2 * I], vi[2 * I]});
      st = cfma(clam, sa, cpx{vr[2 * I + 1], vi[2 * I + 1]});
      hr[I] = pk(sa.r, st.r); hi[I] = pk(sa.i, st.i);
      if constexpr (I == 3) {
        pin(hr[I]); pin(hi[I]);
        SB(); bar(); SB();
        *(LAS u32x4*)(uintptr_t)trw = (u32x4){hr[0], hr[1], hr[2], hr[3]};
        *(LAS u32x4*)(uintptr_t)(trw + 2048) = (u32x4){hi[0], hi[1], hi[2], hi[3]};
        SB();
      }
    });
    pin(hr[7]); pin(hi[7]);
    SB();
    *(LAS u32x4*)(uintptr_t)trw2 = (u32x4){hr[4], hr[5], hr[6], hr[7]};
    *(LAS u32x4*)(uintptr_t)(trw2 + 2048) = (u32x4){hi[4], hi[5], hi[6], hi[7]};
    u32x4 f0 = ldstr(trr, trr2 + 256), f1 = ldstr(trr + 1024, trr2 + 1280);
    u32x4 f2 = ldstr(trr + 2048, trr2 + 2304), f3 = ldstr(trr + 3072, trr2 + 3328);
    pin(f0); pin(f1); pin(f2); pin(f3);
    SB();
    st128_sc1(f0, vAct, rsW, (2 * c) * 1024);
    st128_sc1(f1, vAct, rsW, (1 + 2 * c) * 1024);
    st128_sc1(f2, vAct, rsW, (32 + 2 * c) * 1024);
    st128_sc1(f3, vAct, rsW, (33 + 2 * c) * 1024);
    MARK(M_HST);
    SB();
    issue(IC<29>{});
    waitv<G, 11, last_unit(13)>(); bar();
    SB();
    ffn_block([&](auto J) AI { if constexpr (J == 0) { SB(); waitv<G, 12, last_unit(14)>(); SB(); } },
              [&](auto K) AI {
                constexpr int k = K;
                if constexpr (k == 7) { waitv<G, 33, M_HST>(); flag(flagP + 512); SB(); }
                if constexpr (k >= 23 && k < 31) loadA1(IC<(k - 23) / 2>{});
              });
    loadA(IC<1>{});
    SB();
  }
  const u32 rdD = ks * 8192 + (4 * h + q4) * 128 + 16 * ((2 * a1 + (p4 >> 1)) ^ (4 * (q4 >> 1))) + 8 * (p4 & 1);
  const u32 rdC = ks * 8192 + rdA;
  u32x4 w2[2][2];
  auto rdR = [&](auto K) AI {
    constexpr int k = K, rho = k / 4, s = k % 4; constexpr u32 win = step_slot(16 + rho) * 32768;
    if constexpr (rho < 4) {
      const u32 a = opq((rdC ^ (32 * s)) + win);
      w2[k & 1][0] = lds128(a); w2[k & 1][1] = lds128(a + 4096);
    } else {
      const u32 b0 = opq(rdD + win), b1 = opq((rdD ^ 64) + win);
      w2[k & 1][0] = ldstr(b0 + s * 2048, b0 + s * 2048 + 1024);
      w2[k & 1][1] = ldstr(b1 + s * 2048, b1 + s * 2048 + 1024);
    }
  };
  rdR(IC<0>{});
  waitv<G, 32, M_A0>();
  pinA(IC<0>{});
  SB();
  f32x16 accS[2] = {}, accF[2] = {};
  float g = 0.f;
  f32x4 sk[2];
  auto request = [&](auto Q) AI {
    constexpr int e = rq_e(Q), i = rq_i(Q);
    if constexpr (e < 100) { if constexpr (!(G == 0 && e < 4)) loadA1(IC<4 * e + i>{}); }
    else if constexpr (G == 0) issueP(IC<2 * (16 + e - 100) + i / 2>{}, IC<i % 2>{});
  };
  const u32 vB = heavy ? voffAct : OOB, sB = (128 + 16 * i4 + 4 * sg) * 1024;
  u32x4 frB;
  sfor<44>([&](auto K) AI {
    constexpr int k = K, rho = k / 4, s = k % 4;
    if constexpr (s == 3 && rho < 10) {
      SB();
      waitv<G, 16 + rho, last_unit(17 + rho), M_A0 + rho + 1, (G == 1 && rho == FB) ? M_ACTB : (G == 0 && rho == PA) ? M_POLLA : (G == 0 && rho == PB) ? M_POLLB : M_NONE,
            rho == 5 ? M_MISC : M_NONE>();
      SB();
      if constexpr (G == 1 && rho == FB) { flag(flagP + 256); }
      if constexpr (G == 0 && rho == PA) check(pollA, flagC);
      if constexpr (G == 0 && rho == PB) check(pollB, flagC + 256);
      bar();
      SB();
      if constexpr (G == 0 && rho == PA - 1) { pollA = ld64_sc1<0>(flagC, rsW); MARK(M_POLLA); }
      if constexpr (G == 0 && rho == PB - 1) { pollB = ld64_sc1<256>(flagC, rsW); MARK(M_POLLB); }
      pinA(IC<rho + 1>{});
      if constexpr (rho == 5) {
        pin(gq); pin(xs0); pin(xs1); pin(ds0); pin(ds1);
        if (__builtin_expect(__builtin_amdgcn_ballot_w64(gq.y != tag) != 0, 0)) {
#pragma unroll 1
          for (u32 it = 0; it < kSpinCap; ++it) {
            gq = ld64_slow(OFF_G + (tok0 + 32 * tb2 + tau(n)) * 8, rsW);
            if (__builtin_amdgcn_ballot_w64(gq.y != tag) == 0) break;
            __builtin_amdgcn_s_sleep(1);
          }
        }
        g = __uint_as_float(gq.x);
      }
    }
    if constexpr (k < 43) rdR(IC<k + 1>{});
    if constexpr (rho < 4) { accS[0] = mfma(w2[k & 1][0], af[k], accS[0]); accS[1] = mfma(w2[k & 1][1], af[k], accS[1]); pin(accS[0]); pin(accS[1]); }
    else { accF[0] = mfma(w2[k & 1][0], af[k], accF[0]); accF[1] = mfma(w2[k & 1][1], af[k], accF[1]); pin(accF[0]); pin(accF[1]); }
    SB();
    sfor<rq(k) - rq(k - 1)>([&](auto I) AI { request(IC<rq(k - 1) + I>{}); });
    SB();
    if constexpr (G == 1 && k < 8) {
      sfor<2>([&](auto J) AI {
        constexpr int u = k / 2, i = 2 * (k % 2) + J;
        frB[i] = pk(silu_mul(acc[u][2 * i], acc[u][2 * i + 8]), silu_mul(acc[u][2 * i + 1], acc[u][2 * i + 9]));
      });
      pin(frB);
      if constexpr (k % 2 == 1) st128_sc1(frB, vB, rsW, sB + (k / 2) * 1024);
      if constexpr (k == 7) MARK(M_ACTB);
    }
    if constexpr (G == 1 && k == 13) load_misc();
    if constexpr (k == 36) {
  sfor<2>([&](auto NB) AI {
    const u32x2 xs = NB ? xs1 : xs0, ds = NB ? ds1 : ds0;
    sk[NB][0] = bf2f(xs.x & 0xffffu) * bf2f(ds.x & 0xffffu);
    sk[NB][1] = __uint_as_float(xs.x & 0xffff0000u) * __uint_as_float(ds.x & 0xffff0000u);
    sk[NB][2] = bf2f(xs.y & 0xffffu) * bf2f(ds.y & 0xffffu);
    sk[NB][3] = __uint_as_float(xs.y & 0xffff0000u) * __uint_as_float(ds.y & 0xffff0000u);
    pin(sk[NB]);
  });
    }
    if constexpr (k >= 24 && k < 32) { sfor<4>([&](auto J) AI { constexpr int e = 4 * (k - 24) + J; accS[e / 16][e % 16] *= g; }); pin(accS[(k - 24) / 4]); }
    SB();
  });
  SB();
  const float g1 = rbf(1.f - g);
  const u32 rw = opq(32768 + ks * 16384 + (32 * tb2 + n) * 256 + 16 * (h ^ (n & 7) ^ (8 * (n & 1))));
  sfor<2>([&](auto NB) AI {
    sfor<4>([&](auto A) AI {
      f32x4 v;
      sfor<4>([&](auto J) AI { v[(int)J] = __builtin_fmaf(g1, accF[NB][4 * A + J], accS[NB][4 * A + J]); });
      *(LAS f32x4*)(uintptr_t)(rw ^ (128 * NB + 32 * A)) = v;
    });
  });
  *(LAS float*)(uintptr_t)(98304 + (32 * tb2 + n) * 4) = g;
  bar();
  SB();
  const u32 rr = 32768 + frow * 256 + 16 * ((lane & 7) ^ l3 ^ (8 * (l3 & 1)));
  const rsrc_t rsO = mk(pout, 2048 * 2048);
  const float gf = *(LAS float*)(uintptr_t)(98304 + frow * 4);
  sfor<2>([&](auto NB) AI {
    const u32 a = opq(rr ^ (128 * NB));
    f32x4 v = sk[NB] * gf;
    sfor<4>([&](auto Q) AI { v += *(LAS f32x4*)(uintptr_t)(a + Q * 16384); });
    st128<NB * 128>(__builtin_bit_cast(u32x4, v), ftok * 2048 + fcol * 4, rsO);
  });
  SB();
  asm volatile("; BODY_END %0" ::"i"(G));
}

__global__ __launch_bounds__(512, 2) __attribute__((target("no-packed-fp32-ops"))) void fused_kernel(const void* px, const void* pLre, const void* pLim, const void* pBre, const void* pBim,
                                                       const void* pCre, const void* pCim, const void* pldt, const void* pDsk, const void* pwg,
                                                       const void* pwu, const void* pwd, const void* pgw, float* pout, void* pws) {
  asm volatile("; PRE_BEGIN");
  const u32 w = __builtin_amdgcn_readfirstlane(threadIdx.x >> 6) ^ 4;
  const u32 lane = threadIdx.x & 63, b = blockIdx.x, xcd = b & 7, idx = b >> 3;
  SB();
  unsigned long long pc; asm volatile("s_getpc_b64 %0 ; GETPC" : "=s"(pc));
  const u32 tc = touch(lane < 8 ? ((idx & 3) * 8 + w) * 1024 + lane * 128 : +OOB, mk((const void*)(pc & ~255ull), CODE_AHEAD));
  SB();
  u32 w2 = w; asm volatile("" : "+s"(w2));
  if (w2 < 4) {
    asm volatile("; PRE0_BEGIN");
    const u32 r = 8 * (xcd & 1) + (idx & 7), c = 4 * (xcd >> 1) + (idx >> 3);
    const u32 tb = w & 3, t1 = tb & 1, t2 = tb >> 1, l3 = lane >> 3, swz = 16 * ((lane & 7) ^ (lane >> 4));
    const u32 vX = (128 * r + 32 * tb + 16 * (lane >> 5) + (l3 & 3)) * 1024 + swz, vX1 = (vX ^ 64) + 3072;
    const u32 vB = (32 * c + l3) * 1024 + swz + 128 * t2, vB1 = (vB ^ 64) + 7168;
    const void* const pB = t1 ? pBim : pBre;
    const rsrc_t rsX = mk(px, 2048 * 1024), rsB = mk(pB, 512 * 1024);
    dma4<0, 0, 0, 6144>(tb * 4096, vX, vX1, rsX);
    dma4<1, 16384, 0, 14336>(t2 * 24576 + t1 * 4096, vB, vB1, rsB);
    dma4<2, 24576, 128, 6144>(tb * 4096, vX, vX1, rsX);
    asm volatile("; PRE0_END");
  }
  if (w < 4) body<0>(px, pLre, pLim, pBre, pBim, pCre, pCim, pldt, pDsk, pwg, pwu, pwd, pgw, pout, pws, w, tc);
  else body<1>(px, pLre, pLim, pBre, pBim, pCre, pCim, pldt, pDsk, pwg, pwu, pwd, pgw, pout, pws, w, tc);
}

extern "C" void kernel_launch(void* const* d_in, const int* in_sizes, int n_in, void* d_out, int out_size, void* d_ws, size_t ws_size,
                              hipStream_t stream) {
  (void)in_sizes; (void)n_in; (void)out_size; (void)ws_size;
  (void)hipFuncSetAttribute((const void*)fused_kernel, hipFuncAttributeMaxDynamicSharedMemorySize, LDS_BYTES);
  fused_kernel<<<256, 512, LDS_BYTES, stream>>>(d_in[0], d_in[1], d_in[2], d_in[3], d_in[4], d_in[5], d_in[6], d_in[7], d_in[8], d_in[10],
                                                d_in[11], d_in[12], d_in[13], (float*)d_out, d_ws);
}
```
